```python
import torch

def _softmax(x, axis=-1):
    return torch.softmax(x, dim=axis)
def _gelu(x):
    return torch.nn.functional.gelu(x, approximate="tanh")
def _silu(x):
    return torch.nn.functional.silu(x)
def _sigmoid(x):
    return torch.sigmoid(x)
def _layernorm(x, axis=-1, eps=1e-5):
    m = x.mean(dim=axis, keepdim=True)
    v = x.var(dim=axis, keepdim=True, unbiased=False)
    return (x - m) / torch.sqrt(v + eps)
def _scatter_add(tgt, idx, src, axis=0):
    return tgt.clone().index_add_(axis, idx, src)
def _segment_sum(src, seg, n):
    out = torch.zeros((n,) + src.shape[1:], dtype=src.dtype)
    return out.index_add_(0, seg, src)
def _pad(x, pw):
    flat = tuple(v for pair in reversed(pw) for v in pair)
    return torch.nn.functional.pad(x, flat)

kernel_name = 'standalone__paged_block_table_kv_radixattention__sampling'

def setup_inputs():

    _w = (lambda a: (lambda t: t.to(torch.bfloat16) if t.is_floating_point() else t)(torch.as_tensor(a)))
    B, H, Hkv, Dh = 8, 32, 8, 128
    Lk = 2048
    bs = 16
    nb = max(2, Lk // bs)
    P = max(1, nb // 2)
    n_phys = P + B * (nb - P)
    shared = torch.arange(P, dtype=torch.int64)
    priv = P + torch.arange(B * (nb - P), dtype=torch.int64).reshape(B, nb - P)
    bt = torch.cat([torch.tile(shared[None, :], (B, 1)), priv], dim=1)
    ctx = nb * bs - (torch.arange(1, B + 1, dtype=torch.int64) * 2654435761) % max(1, (nb * bs - P * bs) // 2)
    return {
        "q": _w((lambda *s: torch.randn(*s, dtype=torch.bfloat16) * 0.8)(B, H, Dh)),
        "k_cache": _w((lambda *s: torch.randn(*s, dtype=torch.bfloat16) * 0.8)(n_phys, bs, Hkv, Dh)),
        "v_cache": _w((lambda *s: torch.randn(*s, dtype=torch.bfloat16) * 0.05)(n_phys, bs, Hkv, Dh)),
        "block_table": _w(bt.to(torch.int64)),
        "ctx_lens": _w(torch.as_tensor(ctx).to(torch.int64)),
    }

def reference(q, k_cache, v_cache, block_table, ctx_lens):
    B, H, Dh = q.shape
    n_phys, bs, Hkv, _ = k_cache.shape
    nb = block_table.shape[1]
    Lk = nb * bs
    rep = H // Hkv
    k = k_cache[block_table].reshape(B, Lk, Hkv, Dh)
    v = v_cache[block_table].reshape(B, Lk, Hkv, Dh)
    k = (lambda x, n, axis: torch.repeat_interleave(x, n, dim=axis))(k, rep, axis=2)
    v = (lambda x, n, axis: torch.repeat_interleave(x, n, dim=axis))(v, rep, axis=2)
    scores = torch.einsum("bhd,bkhd->bhk", q, k) * (Dh ** -0.5)
    pos = torch.arange(Lk)
    valid = pos[None, :] < ctx_lens[:, None]
    scores = torch.where(valid[:, None, :], scores, -1e9)
    p = _softmax(scores, axis=-1)
    return torch.einsum("bhk,bkhd->bhd", p, v)
```

```cpp
#include <hip/hip_runtime.h>
#include <hip/hip_bf16.h>
#include <stdint.h>

namespace {

constexpr int B_ = 8, H_ = 32, HKV_ = 8, D_ = 128, BS_ = 16, NB_ = 128, LK_ = NB_ * BS_;
constexpr int G_ = H_ / HKV_;
constexpr int NSPLIT = 4;
constexpr int WAVES = 8;
constexpr int TOKROW = HKV_ * D_;

using bf16x8 = __attribute__((ext_vector_type(8))) __bf16;
using f32x4 = __attribute__((ext_vector_type(4))) float;
using u32x4 = __attribute__((ext_vector_type(4))) unsigned;
using i32x4 = __attribute__((ext_vector_type(4))) int;

__device__ __forceinline__ unsigned cvtpk(float lo, float hi) {
  unsigned r;
  asm volatile("v_cvt_pk_bf16_f32 %0, %1, %2" : "=v"(r) : "v"(lo), "v"(hi));
  return r;
}
__device__ __forceinline__ float xrow_max(float x) {
  auto s = __builtin_amdgcn_permlane16_swap(__float_as_uint(x), __float_as_uint(x), false, false);
  x = fmaxf(__uint_as_float(s[0]), __uint_as_float(s[1]));
  auto t = __builtin_amdgcn_permlane32_swap(__float_as_uint(x), __float_as_uint(x), false, false);
  return fmaxf(__uint_as_float(t[0]), __uint_as_float(t[1]));
}
__device__ __forceinline__ float xrow_sum(float x) {
  auto s = __builtin_amdgcn_permlane16_swap(__float_as_uint(x), __float_as_uint(x), false, false);
  x = __uint_as_float(s[0]) + __uint_as_float(s[1]);
  auto t = __builtin_amdgcn_permlane32_swap(__float_as_uint(x), __float_as_uint(x), false, false);
  return __uint_as_float(t[0]) + __uint_as_float(t[1]);
}
__device__ __forceinline__ u32x4 ld16(const unsigned short* p) {
  return *reinterpret_cast<const u32x4*>(p);
}

constexpr size_t WS_PO = 0;
constexpr size_t WS_PML = (size_t)B_ * H_ * NSPLIT * D_ * 4;

__global__ void __launch_bounds__(WAVES * 64, 2)
attn_split_kernel(const unsigned short* __restrict__ q, const unsigned short* __restrict__ kc,
                  const unsigned short* __restrict__ vc, const int* __restrict__ bt,
                  const int* __restrict__ lens, float* __restrict__ po, float* __restrict__ pml) {
  __shared__ __attribute__((aligned(16))) float s_o[WAVES][G_][D_];
  __shared__ float s_m[WAVES][G_];
  __shared__ float s_l[WAVES][G_];

  const int bid = blockIdx.x;
  const int kvh = bid & 7, b = (bid >> 3) & 7, s = bid >> 6;
  const int tid = threadIdx.x, lane = tid & 63;
  const int w = __builtin_amdgcn_readfirstlane(tid >> 6);
  const int c = lane & 15, g = lane >> 4;
  const int chunk = w * NSPLIT + s;
  const int tok0 = chunk * 64;

  int ctx = lens[b];
  const i32x4 btv = *reinterpret_cast<const i32x4*>(bt + b * NB_ + chunk * 4);
  float c2 = 0.088388347648318440f * 1.4426950408889634f;
  if (ctx <= 0) { ctx = LK_; c2 = 0.f; }
  if (ctx > LK_) ctx = LK_;

  float mrow = -INFINITY, lrow = 0.f;
  f32x4 acc_o[8];
#pragma unroll
  for (int j = 0; j < 8; ++j) acc_o[j] = f32x4{0.f, 0.f, 0.f, 0.f};

  if (tok0 < ctx) {
    int phys[4];
#pragma unroll
    for (int i = 0; i < 4; ++i) phys[i] = (tok0 + i * 16 < ctx) ? btv[i] : btv[0];
    u32x4 kf[4][4];
#pragma unroll
    for (int i = 0; i < 4; ++i) {
      const unsigned short* kb = kc + ((size_t)phys[i] * BS_ + c) * TOKROW + kvh * D_ + g * 8;
#pragma unroll
      for (int j = 0; j < 4; ++j) kf[i][j] = ld16(kb + j * 32);
    }
    u32x4 qf[4];
    {
      const unsigned short* qb = q + ((size_t)b * H_ + kvh * G_ + (c & 3)) * D_ + g * 8;
#pragma unroll
      for (int j = 0; j < 4; ++j) qf[j] = ld16(qb + j * 32);
    }
    u32x4 vf[2][8];
#pragma unroll
    for (int p = 0; p < 2; ++p)
#pragma unroll
      for (int i = 0; i < 8; ++i) {
        const unsigned short* vb = vc + ((size_t)phys[2 * p + (i >> 2)] * BS_ + g * 4 + (i & 3)) * TOKROW + kvh * D_ + c * 8;
        vf[p][i] = ld16(vb);
      }
    f32x4 sc[4];
#pragma unroll
    for (int i = 0; i < 4; ++i) {
      f32x4 a = f32x4{0.f, 0.f, 0.f, 0.f};
#pragma unroll
      for (int j = 0; j < 4; ++j)
        a = __builtin_amdgcn_mfma_f32_16x16x32_bf16(__builtin_bit_cast(bf16x8, kf[i][j]), __builtin_bit_cast(bf16x8, qf[j]), a, 0, 0, 0);
      sc[i] = a;
    }
    float mx = -INFINITY;
#pragma unroll
    for (int i = 0; i < 4; ++i)
#pragma unroll
      for (int r = 0; r < 4; ++r) {
        const bool live = (tok0 + i * 16 + g * 4 + r) < ctx;
        const float x = live ? sc[i][r] * c2 : -INFINITY;
        sc[i][r] = x;
        mx = fmaxf(mx, x);
      }
    mx = xrow_max(mx);
    float ls = 0.f;
#pragma unroll
    for (int i = 0; i < 4; ++i)
#pragma unroll
      for (int r = 0; r < 4; ++r) {
        const float e = __builtin_amdgcn_exp2f(sc[i][r] - mx);
        sc[i][r] = e;
        ls += e;
      }
    ls = xrow_sum(ls);
    mrow = mx;
    lrow = ls;
#pragma unroll
    for (int p = 0; p < 2; ++p) {
      u32x4 pf;
      pf[0] = cvtpk(sc[2 * p][0], sc[2 * p][1]);
      pf[1] = cvtpk(sc[2 * p][2], sc[2 * p][3]);
      pf[2] = cvtpk(sc[2 * p + 1][0], sc[2 * p + 1][1]);
      pf[3] = cvtpk(sc[2 * p + 1][2], sc[2 * p + 1][3]);
#pragma unroll
      for (int j = 0; j < 8; ++j) {
        u32x4 bfr;
#pragma unroll
        for (int u = 0; u < 4; ++u) {
          const unsigned lo = vf[p][2 * u][j >> 1], hi = vf[p][2 * u + 1][j >> 1];
          bfr[u] = (j & 1) ? __builtin_amdgcn_perm(hi, lo, 0x07060302u) : __builtin_amdgcn_perm(hi, lo, 0x05040100u);
        }
        acc_o[j] = __builtin_amdgcn_mfma_f32_16x16x32_bf16(__builtin_bit_cast(bf16x8, pf), __builtin_bit_cast(bf16x8, bfr), acc_o[j], 0, 0, 0);
      }
    }
  }
  if (g == 0) {
#pragma unroll
    for (int r = 0; r < 4; ++r) {
      *reinterpret_cast<f32x4*>(&s_o[w][r][c * 8]) = f32x4{acc_o[0][r], acc_o[1][r], acc_o[2][r], acc_o[3][r]};
      *reinterpret_cast<f32x4*>(&s_o[w][r][c * 8 + 4]) = f32x4{acc_o[4][r], acc_o[5][r], acc_o[6][r], acc_o[7][r]};
    }
    if (c < 4) { s_m[w][c] = mrow; s_l[w][c] = lrow; }
  }
  __syncthreads();
  {
    const int h = tid >> 7, d = tid & 127;
    float M = -INFINITY;
#pragma unroll
    for (int ww = 0; ww < WAVES; ++ww) M = fmaxf(M, s_m[ww][h]);
    float L = 0.f, o = 0.f;
#pragma unroll
    for (int ww = 0; ww < WAVES; ++ww) {
      const float lw = s_l[ww][h];
      const float wt = (lw > 0.f) ? __builtin_amdgcn_exp2f(s_m[ww][h] - M) : 0.f;
      L = fmaf(wt, lw, L);
      o = fmaf(wt, s_o[ww][h][d], o);
    }
    const size_t slot = ((size_t)b * H_ + kvh * G_ + h) * NSPLIT + s;
    po[slot * D_ + d] = o;
    if (d == 0) { pml[slot * 2] = M; pml[slot * 2 + 1] = L; }
  }
}

__global__ void __launch_bounds__(256)
attn_combine_kernel(const float* __restrict__ po, const float* __restrict__ pml, unsigned* __restrict__ out) {
  const int bid = blockIdx.x;
  const int row = bid * G_ + (threadIdx.x >> 6);
  const int d0 = (threadIdx.x & 63) * 2;
  float m[NSPLIT], l[NSPLIT];
  float2 a[NSPLIT];
#pragma unroll
  for (int s = 0; s < NSPLIT; ++s) {
    const float2 ml = *reinterpret_cast<const float2*>(pml + ((size_t)row * NSPLIT + s) * 2);
    m[s] = ml.x; l[s] = ml.y;
    a[s] = *reinterpret_cast<const float2*>(po + ((size_t)row * NSPLIT + s) * D_ + d0);
  }
  float M = -INFINITY;
#pragma unroll
  for (int s = 0; s < NSPLIT; ++s) M = fmaxf(M, m[s]);
  float L = 0.f, o0 = 0.f, o1 = 0.f;
#pragma unroll
  for (int s = 0; s < NSPLIT; ++s) {
    const float wt = (l[s] > 0.f) ? __builtin_amdgcn_exp2f(m[s] - M) : 0.f;
    L = fmaf(wt, l[s], L);
    o0 = fmaf(wt, a[s].x, o0);
    o1 = fmaf(wt, a[s].y, o1);
  }
  const float inv = 1.f / L;
  out[(size_t)row * (D_ / 2) + (d0 >> 1)] = cvtpk(o0 * inv, o1 * inv);
}

}

extern "C" void kernel_launch(void* const* d_in, const int* in_sizes, int n_in,
                              void* d_out, int out_size, void* d_ws, size_t ws_size,
                              hipStream_t stream) {
  const unsigned short* q = (const unsigned short*)d_in[0];
  const unsigned short* kc = (const unsigned short*)d_in[1];
  const unsigned short* vc = (const unsigned short*)d_in[2];
  const int* bt = (const int*)d_in[3];
  const int* lens = (const int*)d_in[4];
  float* po = (float*)((char*)d_ws + WS_PO);
  float* pml = (float*)((char*)d_ws + WS_PML);
  attn_split_kernel<<<dim3(B_ * HKV_ * NSPLIT), dim3(WAVES * 64), 0, stream>>>(q, kc, vc, bt, lens, po, pml);
  attn_combine_kernel<<<dim3(B_ * HKV_), dim3(256), 0, stream>>>(po, pml, (unsigned*)d_out);
}
```

```cpp
#include <hip/hip_runtime.h>
#include <hip/hip_bf16.h>
#include <stdint.h>

namespace {

constexpr int B_ = 8, H_ = 32, HKV_ = 8, D_ = 128, BS_ = 16, NB_ = 128, LK_ = NB_ * BS_;
constexpr int G_ = H_ / HKV_;
constexpr int NSPLIT = 4;
constexpr int WAVES = 8;
constexpr int TOKROW = HKV_ * D_;

using bf16x8 = __attribute__((ext_vector_type(8))) __bf16;
using f32x4 = __attribute__((ext_vector_type(4))) float;
using u32x4 = __attribute__((ext_vector_type(4))) unsigned;
using i32x4 = __attribute__((ext_vector_type(4))) int;

__device__ __forceinline__ unsigned cvtpk(float lo, float hi) {
  unsigned r;
  asm volatile("v_cvt_pk_bf16_f32 %0, %1, %2" : "=v"(r) : "v"(lo), "v"(hi));
  return r;
}
__device__ __forceinline__ float xrow_max(float x) {
  auto s = __builtin_amdgcn_permlane16_swap(__float_as_uint(x), __float_as_uint(x), false, false);
  x = fmaxf(__uint_as_float(s[0]), __uint_as_float(s[1]));
  auto t = __builtin_amdgcn_permlane32_swap(__float_as_uint(x), __float_as_uint(x), false, false);
  return fmaxf(__uint_as_float(t[0]), __uint_as_float(t[1]));
}
__device__ __forceinline__ float xrow_sum(float x) {
  auto s = __builtin_amdgcn_permlane16_swap(__float_as_uint(x), __float_as_uint(x), false, false);
  x = __uint_as_float(s[0]) + __uint_as_float(s[1]);
  auto t = __builtin_amdgcn_permlane32_swap(__float_as_uint(x), __float_as_uint(x), false, false);
  return __uint_as_float(t[0]) + __uint_as_float(t[1]);
}
__device__ __forceinline__ u32x4 ld16(const unsigned short* p) {
  return __builtin_nontemporal_load(reinterpret_cast<const u32x4*>(p));
}

constexpr size_t WS_PO = 0;
constexpr size_t WS_PML = (size_t)B_ * H_ * NSPLIT * D_ * 4;

__global__ void __launch_bounds__(WAVES * 64, 2)
attn_split_kernel(const unsigned short* __restrict__ q, const unsigned short* __restrict__ kc,
                  const unsigned short* __restrict__ vc, const int* __restrict__ bt,
                  const int* __restrict__ lens, float* __restrict__ po, float* __restrict__ pml) {
  __shared__ __attribute__((aligned(16))) float s_o[WAVES][G_][D_];
  __shared__ float s_m[WAVES][G_];
  __shared__ float s_l[WAVES][G_];

  const int bid = blockIdx.x;
  const int kvh = bid & 7, b = (bid >> 3) & 7, s = bid >> 6;
  const int tid = threadIdx.x, lane = tid & 63;
  const int w = __builtin_amdgcn_readfirstlane(tid >> 6);
  const int c = lane & 15, g = lane >> 4;
  const int chunk = w * NSPLIT + s;
  const int tok0 = chunk * 64;

  int ctx = lens[b];
  const i32x4 btv = *reinterpret_cast<const i32x4*>(bt + b * NB_ + chunk * 4);
  u32x4 qf[4];
  {
    const unsigned short* qb = q + ((size_t)b * H_ + kvh * G_ + (c & 3)) * D_ + g * 8;
#pragma unroll
    for (int j = 0; j < 4; ++j) qf[j] = ld16(qb + j * 32);
  }
  __builtin_amdgcn_sched_barrier(0);
  asm volatile("" ::"s"(ctx), "s"(btv[0]), "s"(btv[1]), "s"(btv[2]), "s"(btv[3]));
  float c2 = 0.088388347648318440f * 1.4426950408889634f;
  if (ctx <= 0) { ctx = LK_; c2 = 0.f; }
  if (ctx > LK_) ctx = LK_;

  float mrow = -INFINITY, lrow = 0.f;
  f32x4 acc_o[8];
#pragma unroll
  for (int j = 0; j < 8; ++j) acc_o[j] = f32x4{0.f, 0.f, 0.f, 0.f};

  if (tok0 < ctx) {
    int phys[4];
#pragma unroll
    for (int i = 0; i < 4; ++i) phys[i] = (tok0 + i * 16 < ctx) ? btv[i] : btv[0];
    u32x4 kf[4][4];
#pragma unroll
    for (int i = 0; i < 4; ++i) {
      const unsigned short* kb = kc + ((size_t)phys[i] * BS_ + c) * TOKROW + kvh * D_ + g * 8;
#pragma unroll
      for (int j = 0; j < 4; ++j) kf[i][j] = ld16(kb + j * 32);
    }
    u32x4 vf[2][8];
#pragma unroll
    for (int p = 0; p < 2; ++p)
#pragma unroll
      for (int i = 0; i < 8; ++i) {
        const unsigned short* vb = vc + ((size_t)phys[2 * p + (i >> 2)] * BS_ + g * 4 + (i & 3)) * TOKROW + kvh * D_ + c * 8;
        vf[p][i] = ld16(vb);
      }
    __builtin_amdgcn_sched_barrier(0);
    f32x4 sc[4];
#pragma unroll
    for (int i = 0; i < 4; ++i) {
      f32x4 a = f32x4{0.f, 0.f, 0.f, 0.f};
#pragma unroll
      for (int j = 0; j < 4; ++j)
        a = __builtin_amdgcn_mfma_f32_16x16x32_bf16(__builtin_bit_cast(bf16x8, kf[i][j]), __builtin_bit_cast(bf16x8, qf[j]), a, 0, 0, 0);
      sc[i] = a;
    }
    float mx = -INFINITY;
#pragma unroll
    for (int i = 0; i < 4; ++i)
#pragma unroll
      for (int r = 0; r < 4; ++r) {
        const bool live = (tok0 + i * 16 + g * 4 + r) < ctx;
        const float x = live ? sc[i][r] * c2 : -INFINITY;
        sc[i][r] = x;
        mx = fmaxf(mx, x);
      }
    mx = xrow_max(mx);
    float ls = 0.f;
#pragma unroll
    for (int i = 0; i < 4; ++i)
#pragma unroll
      for (int r = 0; r < 4; ++r) {
        const float e = __builtin_amdgcn_exp2f(sc[i][r] - mx);
        sc[i][r] = e;
        ls += e;
      }
    ls = xrow_sum(ls);
    mrow = mx;
    lrow = ls;
#pragma unroll
    for (int p = 0; p < 2; ++p) {
      u32x4 pf;
      pf[0] = cvtpk(sc[2 * p][0], sc[2 * p][1]);
      pf[1] = cvtpk(sc[2 * p][2], sc[2 * p][3]);
      pf[2] = cvtpk(sc[2 * p + 1][0], sc[2 * p + 1][1]);
      pf[3] = cvtpk(sc[2 * p + 1][2], sc[2 * p + 1][3]);
#pragma unroll
      for (int j = 0; j < 8; ++j) {
        u32x4 bfr;
#pragma unroll
        for (int u = 0; u < 4; ++u) {
          const unsigned lo = vf[p][2 * u][j >> 1], hi = vf[p][2 * u + 1][j >> 1];
          bfr[u] = (j & 1) ? __builtin_amdgcn_perm(hi, lo, 0x07060302u) : __builtin_amdgcn_perm(hi, lo, 0x05040100u);
        }
        acc_o[j] = __builtin_amdgcn_mfma_f32_16x16x32_bf16(__builtin_bit_cast(bf16x8, pf), __builtin_bit_cast(bf16x8, bfr), acc_o[j], 0, 0, 0);
      }
    }
  }
  if (g == 0) {
#pragma unroll
    for (int r = 0; r < 4; ++r) {
      *reinterpret_cast<f32x4*>(&s_o[w][r][c * 8]) = f32x4{acc_o[0][r], acc_o[1][r], acc_o[2][r], acc_o[3][r]};
      *reinterpret_cast<f32x4*>(&s_o[w][r][c * 8 + 4]) = f32x4{acc_o[4][r], acc_o[5][r], acc_o[6][r], acc_o[7][r]};
    }
    if (c < 4) { s_m[w][c] = mrow; s_l[w][c] = lrow; }
  }
  __syncthreads();
  {
    const int h = tid >> 7, d = tid & 127;
    float M = -INFINITY;
#pragma unroll
    for (int ww = 0; ww < WAVES; ++ww) M = fmaxf(M, s_m[ww][h]);
    float L = 0.f, o = 0.f;
#pragma unroll
    for (int ww = 0; ww < WAVES; ++ww) {
      const float lw = s_l[ww][h];
      const float wt = (lw > 0.f) ? __builtin_amdgcn_exp2f(s_m[ww][h] - M) : 0.f;
      L = fmaf(wt, lw, L);
      o = fmaf(wt, s_o[ww][h][d], o);
    }
    const size_t slot = ((size_t)b * H_ + kvh * G_ + h) * NSPLIT + s;
    po[slot * D_ + d] = o;
    if (d == 0) { pml[slot * 2] = M; pml[slot * 2 + 1] = L; }
  }
}

__global__ void __launch_bounds__(256)
attn_combine_kernel(const float* __restrict__ po, const float* __restrict__ pml, unsigned* __restrict__ out) {
  const int bid = blockIdx.x;
  const int row = bid * G_ + (threadIdx.x >> 6);
  const int d0 = (threadIdx.x & 63) * 2;
  float m[NSPLIT], l[NSPLIT];
  float2 a[NSPLIT];
#pragma unroll
  for (int s = 0; s < NSPLIT; ++s) {
    const float2 ml = *reinterpret_cast<const float2*>(pml + ((size_t)row * NSPLIT + s) * 2);
    m[s] = ml.x; l[s] = ml.y;
    a[s] = *reinterpret_cast<const float2*>(po + ((size_t)row * NSPLIT + s) * D_ + d0);
  }
  float M = -INFINITY;
#pragma unroll
  for (int s = 0; s < NSPLIT; ++s) M = fmaxf(M, m[s]);
  float L = 0.f, o0 = 0.f, o1 = 0.f;
#pragma unroll
  for (int s = 0; s < NSPLIT; ++s) {
    const float wt = (l[s] > 0.f) ? __builtin_amdgcn_exp2f(m[s] - M) : 0.f;
    L = fmaf(wt, l[s], L);
    o0 = fmaf(wt, a[s].x, o0);
    o1 = fmaf(wt, a[s].y, o1);
  }
  const float inv = 1.f / L;
  out[(size_t)row * (D_ / 2) + (d0 >> 1)] = cvtpk(o0 * inv, o1 * inv);
}

}

extern "C" void kernel_launch(void* const* d_in, const int* in_sizes, int n_in,
                              void* d_out, int out_size, void* d_ws, size_t ws_size,
                              hipStream_t stream) {
  const unsigned short* q = (const unsigned short*)d_in[0];
  const unsigned short* kc = (const unsigned short*)d_in[1];
  const unsigned short* vc = (const unsigned short*)d_in[2];
  const int* bt = (const int*)d_in[3];
  const int* lens = (const int*)d_in[4];
  float* po = (float*)((char*)d_ws + WS_PO);
  float* pml = (float*)((char*)d_ws + WS_PML);
  attn_split_kernel<<<dim3(B_ * HKV_ * NSPLIT), dim3(WAVES * 64), 0, stream>>>(q, kc, vc, bt, lens, po, pml);
  attn_combine_kernel<<<dim3(B_ * HKV_), dim3(256), 0, stream>>>(po, pml, (unsigned*)d_out);
}
```

```cpp
#include <hip/hip_runtime.h>
#include <hip/hip_bf16.h>
#include <stdint.h>

namespace {

constexpr int B_ = 8, H_ = 32, HKV_ = 8, D_ = 128, BS_ = 16, NB_ = 128, LK_ = NB_ * BS_;
constexpr int G_ = H_ / HKV_;
constexpr int NSPLIT = 4;
constexpr int WAVES = 8;
constexpr int TOKROW = HKV_ * D_;

using bf16x8 = __attribute__((ext_vector_type(8))) __bf16;
using f32x4 = __attribute__((ext_vector_type(4))) float;
using u32x4 = __attribute__((ext_vector_type(4))) unsigned;
using i32x4 = __attribute__((ext_vector_type(4))) int;

__device__ __forceinline__ unsigned cvtpk(float lo, float hi) {
  unsigned r;
  asm volatile("v_cvt_pk_bf16_f32 %0, %1, %2" : "=v"(r) : "v"(lo), "v"(hi));
  return r;
}
__device__ __forceinline__ float xrow_max(float x) {
  auto s = __builtin_amdgcn_permlane16_swap(__float_as_uint(x), __float_as_uint(x), false, false);
  x = fmaxf(__uint_as_float(s[0]), __uint_as_float(s[1]));
  auto t = __builtin_amdgcn_permlane32_swap(__float_as_uint(x), __float_as_uint(x), false, false);
  return fmaxf(__uint_as_float(t[0]), __uint_as_float(t[1]));
}
__device__ __forceinline__ float xrow_sum(float x) {
  auto s = __builtin_amdgcn_permlane16_swap(__float_as_uint(x), __float_as_uint(x), false, false);
  x = __uint_as_float(s[0]) + __uint_as_float(s[1]);
  auto t = __builtin_amdgcn_permlane32_swap(__float_as_uint(x), __float_as_uint(x), false, false);
  return __uint_as_float(t[0]) + __uint_as_float(t[1]);
}
__device__ __forceinline__ u32x4 ld16(const unsigned short* p) {
  return __builtin_nontemporal_load(reinterpret_cast<const u32x4*>(p));
}

constexpr size_t WS_PO = 0;
constexpr size_t WS_PML = (size_t)B_ * H_ * NSPLIT * D_ * 4;

__global__ void __launch_bounds__(WAVES * 64, 2)
attn_split_kernel(const unsigned short* __restrict__ q, const unsigned short* __restrict__ kc,
                  const unsigned short* __restrict__ vc, const int* __restrict__ bt,
                  const int* __restrict__ lens, float* __restrict__ po, float* __restrict__ pml) {
  __shared__ __attribute__((aligned(16))) float s_o[WAVES][G_][D_];
  __shared__ float s_m[WAVES][G_];
  __shared__ float s_l[WAVES][G_];

  const int bid = blockIdx.x;
  const int kvh = bid & 7, b = (bid >> 3) & 7, s = bid >> 6;
  const int tid = threadIdx.x, lane = tid & 63;
  const int w = __builtin_amdgcn_readfirstlane(tid >> 6);
  const int c = lane & 15, g = lane >> 4;
  const int chunk = w * NSPLIT + s;
  const int tok0 = chunk * 64;

  unsigned pfv = 0;
  if (w == 0) {
    unsigned long long pc;
    asm volatile("s_getpc_b64 %0" : "=s"(pc));
    unsigned long long ad = pc + (lane < 48 ? lane : 47) * 64;
    asm volatile("global_load_dword %0, %1, off" : "=v"(pfv) : "v"(ad) : "memory");
  }
  int ctx = lens[b];
  const i32x4 btv = *reinterpret_cast<const i32x4*>(bt + b * NB_ + chunk * 4);
  u32x4 qf[4];
  {
    const unsigned short* qb = q + ((size_t)b * H_ + kvh * G_ + (c & 3)) * D_ + g * 8;
#pragma unroll
    for (int j = 0; j < 4; ++j) qf[j] = ld16(qb + j * 32);
  }
  __builtin_amdgcn_sched_barrier(0);
  asm volatile("" ::"s"(ctx), "s"(btv[0]), "s"(btv[1]), "s"(btv[2]), "s"(btv[3]));
  float c2 = 0.088388347648318440f * 1.4426950408889634f;
  if (ctx <= 0) { ctx = LK_; c2 = 0.f; }
  if (ctx > LK_) ctx = LK_;

  float mrow = -INFINITY, lrow = 0.f;
  f32x4 acc_o[8];
#pragma unroll
  for (int j = 0; j < 8; ++j) acc_o[j] = f32x4{0.f, 0.f, 0.f, 0.f};

  if (tok0 < ctx) {
    int phys[4];
#pragma unroll
    for (int i = 0; i < 4; ++i) phys[i] = (tok0 + i * 16 < ctx) ? btv[i] : btv[0];
    u32x4 kf[4][4];
#pragma unroll
    for (int i = 0; i < 4; ++i) {
      const unsigned short* kb = kc + ((size_t)phys[i] * BS_ + c) * TOKROW + kvh * D_ + g * 8;
#pragma unroll
      for (int j = 0; j < 4; ++j) kf[i][j] = ld16(kb + j * 32);
    }
    u32x4 vf[2][8];
#pragma unroll
    for (int p = 0; p < 2; ++p)
#pragma unroll
      for (int i = 0; i < 8; ++i) {
        const unsigned short* vb = vc + ((size_t)phys[2 * p + (i >> 2)] * BS_ + g * 4 + (i & 3)) * TOKROW + kvh * D_ + c * 8;
        vf[p][i] = ld16(vb);
      }
    __builtin_amdgcn_sched_barrier(0);
    f32x4 sc[4];
#pragma unroll
    for (int i = 0; i < 4; ++i) {
      f32x4 a = f32x4{0.f, 0.f, 0.f, 0.f};
#pragma unroll
      for (int j = 0; j < 4; ++j)
        a = __builtin_amdgcn_mfma_f32_16x16x32_bf16(__builtin_bit_cast(bf16x8, kf[i][j]), __builtin_bit_cast(bf16x8, qf[j]), a, 0, 0, 0);
      sc[i] = a;
    }
    float mx = -INFINITY;
#pragma unroll
    for (int i = 0; i < 4; ++i)
#pragma unroll
      for (int r = 0; r < 4; ++r) {
        const bool live = (tok0 + i * 16 + g * 4 + r) < ctx;
        const float x = live ? sc[i][r] * c2 : -INFINITY;
        sc[i][r] = x;
        mx = fmaxf(mx, x);
      }
    mx = xrow_max(mx);
    float ls = 0.f;
#pragma unroll
    for (int i = 0; i < 4; ++i)
#pragma unroll
      for (int r = 0; r < 4; ++r) {
        const float e = __builtin_amdgcn_exp2f(sc[i][r] - mx);
        sc[i][r] = e;
        ls += e;
      }
    ls = xrow_sum(ls);
    mrow = mx;
    lrow = ls;
#pragma unroll
    for (int p = 0; p < 2; ++p) {
      u32x4 pf;
      pf[0] = cvtpk(sc[2 * p][0], sc[2 * p][1]);
      pf[1] = cvtpk(sc[2 * p][2], sc[2 * p][3]);
      pf[2] = cvtpk(sc[2 * p + 1][0], sc[2 * p + 1][1]);
      pf[3] = cvtpk(sc[2 * p + 1][2], sc[2 * p + 1][3]);
#pragma unroll
      for (int j = 0; j < 8; ++j) {
        u32x4 bfr;
#pragma unroll
        for (int u = 0; u < 4; ++u) {
          const unsigned lo = vf[p][2 * u][j >> 1], hi = vf[p][2 * u + 1][j >> 1];
          bfr[u] = (j & 1) ? __builtin_amdgcn_perm(hi, lo, 0x07060302u) : __builtin_amdgcn_perm(hi, lo, 0x05040100u);
        }
        acc_o[j] = __builtin_amdgcn_mfma_f32_16x16x32_bf16(__builtin_bit_cast(bf16x8, pf), __builtin_bit_cast(bf16x8, bfr), acc_o[j], 0, 0, 0);
      }
    }
  }
  if (g == 0) {
#pragma unroll
    for (int r = 0; r < 4; ++r) {
      *reinterpret_cast<f32x4*>(&s_o[w][r][c * 8]) = f32x4{acc_o[0][r], acc_o[1][r], acc_o[2][r], acc_o[3][r]};
      *reinterpret_cast<f32x4*>(&s_o[w][r][c * 8 + 4]) = f32x4{acc_o[4][r], acc_o[5][r], acc_o[6][r], acc_o[7][r]};
    }
    if (c < 4) { s_m[w][c] = mrow; s_l[w][c] = lrow; }
  }
  __syncthreads();
  {
    const int h = tid >> 7, d = tid & 127;
    float M = -INFINITY;
#pragma unroll
    for (int ww = 0; ww < WAVES; ++ww) M = fmaxf(M, s_m[ww][h]);
    float L = 0.f, o = 0.f;
#pragma unroll
    for (int ww = 0; ww < WAVES; ++ww) {
      const float lw = s_l[ww][h];
      const float wt = (lw > 0.f) ? __builtin_amdgcn_exp2f(s_m[ww][h] - M) : 0.f;
      L = fmaf(wt, lw, L);
      o = fmaf(wt, s_o[ww][h][d], o);
    }
    const size_t slot = ((size_t)b * H_ + kvh * G_ + h) * NSPLIT + s;
    po[slot * D_ + d] = o;
    if (d == 0) { pml[slot * 2] = M; pml[slot * 2 + 1] = L; }
  }
  asm volatile("s_waitcnt vmcnt(0)" :: "v"(pfv) : "memory");
}

__global__ void __launch_bounds__(256)
attn_combine_kernel(const float* __restrict__ po, const float* __restrict__ pml, unsigned* __restrict__ out) {
  const int bid = blockIdx.x;
  const int row = bid * G_ + (threadIdx.x >> 6);
  const int d0 = (threadIdx.x & 63) * 2;
  float m[NSPLIT], l[NSPLIT];
  float2 a[NSPLIT];
#pragma unroll
  for (int s = 0; s < NSPLIT; ++s) {
    const float2 ml = *reinterpret_cast<const float2*>(pml + ((size_t)row * NSPLIT + s) * 2);
    m[s] = ml.x; l[s] = ml.y;
    a[s] = *reinterpret_cast<const float2*>(po + ((size_t)row * NSPLIT + s) * D_ + d0);
  }
  float M = -INFINITY;
#pragma unroll
  for (int s = 0; s < NSPLIT; ++s) M = fmaxf(M, m[s]);
  float L = 0.f, o0 = 0.f, o1 = 0.f;
#pragma unroll
  for (int s = 0; s < NSPLIT; ++s) {
    const float wt = (l[s] > 0.f) ? __builtin_amdgcn_exp2f(m[s] - M) : 0.f;
    L = fmaf(wt, l[s], L);
    o0 = fmaf(wt, a[s].x, o0);
    o1 = fmaf(wt, a[s].y, o1);
  }
  const float inv = 1.f / L;
  out[(size_t)row * (D_ / 2) + (d0 >> 1)] = cvtpk(o0 * inv, o1 * inv);
}

}

extern "C" void kernel_launch(void* const* d_in, const int* in_sizes, int n_in,
                              void* d_out, int out_size, void* d_ws, size_t ws_size,
                              hipStream_t stream) {
  const unsigned short* q = (const unsigned short*)d_in[0];
  const unsigned short* kc = (const unsigned short*)d_in[1];
  const unsigned short* vc = (const unsigned short*)d_in[2];
  const int* bt = (const int*)d_in[3];
  const int* lens = (const int*)d_in[4];
  float* po = (float*)((char*)d_ws + WS_PO);
  float* pml = (float*)((char*)d_ws + WS_PML);
  attn_split_kernel<<<dim3(B_ * HKV_ * NSPLIT), dim3(WAVES * 64), 0, stream>>>(q, kc, vc, bt, lens, po, pml);
  attn_combine_kernel<<<dim3(B_ * HKV_), dim3(256), 0, stream>>>(po, pml, (unsigned*)d_out);
}
```

```cpp
#include <hip/hip_runtime.h>
#include <hip/hip_bf16.h>
#include <stdint.h>

namespace {

constexpr int B_ = 8, H_ = 32, HKV_ = 8, D_ = 128, BS_ = 16, NB_ = 128, LK_ = NB_ * BS_;
constexpr int G_ = H_ / HKV_;
constexpr int NSPLIT = 4;
constexpr int WAVES = 8;
constexpr int TOKROW = HKV_ * D_;

using bf16x8 = __attribute__((ext_vector_type(8))) __bf16;
using f32x4 = __attribute__((ext_vector_type(4))) float;
using u32x4 = __attribute__((ext_vector_type(4))) unsigned;
using i32x4 = __attribute__((ext_vector_type(4))) int;

__device__ __forceinline__ unsigned cvtpk(float lo, float hi) {
  unsigned r;
  asm volatile("v_cvt_pk_bf16_f32 %0, %1, %2" : "=v"(r) : "v"(lo), "v"(hi));
  return r;
}
__device__ __forceinline__ float xrow_max(float x) {
  auto s = __builtin_amdgcn_permlane16_swap(__float_as_uint(x), __float_as_uint(x), false, false);
  x = fmaxf(__uint_as_float(s[0]), __uint_as_float(s[1]));
  auto t = __builtin_amdgcn_permlane32_swap(__float_as_uint(x), __float_as_uint(x), false, false);
  return fmaxf(__uint_as_float(t[0]), __uint_as_float(t[1]));
}
__device__ __forceinline__ float xrow_sum(float x) {
  auto s = __builtin_amdgcn_permlane16_swap(__float_as_uint(x), __float_as_uint(x), false, false);
  x = __uint_as_float(s[0]) + __uint_as_float(s[1]);
  auto t = __builtin_amdgcn_permlane32_swap(__float_as_uint(x), __float_as_uint(x), false, false);
  return __uint_as_float(t[0]) + __uint_as_float(t[1]);
}
__device__ __forceinline__ u32x4 ld16(const unsigned short* p) {
  return __builtin_nontemporal_load(reinterpret_cast<const u32x4*>(p));
}

constexpr size_t WS_PO = 0;
constexpr size_t WS_PML = (size_t)B_ * H_ * NSPLIT * D_ * 4;

__global__ void __launch_bounds__(WAVES * 64, 2)
attn_split_kernel(const unsigned short* __restrict__ q, const unsigned short* __restrict__ kc,
                  const unsigned short* __restrict__ vc, const int* __restrict__ bt,
                  const int* __restrict__ lens, float* __restrict__ po, float* __restrict__ pml, unsigned long long* tp) {
  if (blockIdx.x == 0 && threadIdx.x == 0) tp[0] = __builtin_amdgcn_s_memrealtime();
  __shared__ __attribute__((aligned(16))) float s_o[WAVES][G_][D_];
  __shared__ float s_m[WAVES][G_];
  __shared__ float s_l[WAVES][G_];

  const int bid = blockIdx.x;
  const int kvh = bid & 7, b = (bid >> 3) & 7, s = bid >> 6;
  const int tid = threadIdx.x, lane = tid & 63;
  const int w = __builtin_amdgcn_readfirstlane(tid >> 6);
  const int c = lane & 15, g = lane >> 4;
  const int chunk = w * NSPLIT + s;
  const int tok0 = chunk * 64;

  int ctx = lens[b];
  const i32x4 btv = *reinterpret_cast<const i32x4*>(bt + b * NB_ + chunk * 4);
  u32x4 qf[4];
  {
    const unsigned short* qb = q + ((size_t)b * H_ + kvh * G_ + (c & 3)) * D_ + g * 8;
#pragma unroll
    for (int j = 0; j < 4; ++j) qf[j] = ld16(qb + j * 32);
  }
  __builtin_amdgcn_sched_barrier(0);
  asm volatile("" ::"s"(ctx), "s"(btv[0]), "s"(btv[1]), "s"(btv[2]), "s"(btv[3]));
  float c2 = 0.088388347648318440f * 1.4426950408889634f;
  if (ctx <= 0) { ctx = LK_; c2 = 0.f; }
  if (ctx > LK_) ctx = LK_;

  float mrow = -INFINITY, lrow = 0.f;
  f32x4 acc_o[8];
#pragma unroll
  for (int j = 0; j < 8; ++j) acc_o[j] = f32x4{0.f, 0.f, 0.f, 0.f};

  if (tok0 < ctx) {
    int phys[4];
#pragma unroll
    for (int i = 0; i < 4; ++i) phys[i] = (tok0 + i * 16 < ctx) ? btv[i] : btv[0];
    u32x4 kf[4][4];
#pragma unroll
    for (int i = 0; i < 4; ++i) {
      const unsigned short* kb = kc + ((size_t)phys[i] * BS_ + c) * TOKROW + kvh * D_ + g * 8;
#pragma unroll
      for (int j = 0; j < 4; ++j) kf[i][j] = ld16(kb + j * 32);
    }
    u32x4 vf[2][8];
#pragma unroll
    for (int p = 0; p < 2; ++p)
#pragma unroll
      for (int i = 0; i < 8; ++i) {
        const unsigned short* vb = vc + ((size_t)phys[2 * p + (i >> 2)] * BS_ + g * 4 + (i & 3)) * TOKROW + kvh * D_ + c * 8;
        vf[p][i] = ld16(vb);
      }
    __builtin_amdgcn_sched_barrier(0);
    f32x4 sc[4];
#pragma unroll
    for (int i = 0; i < 4; ++i) {
      f32x4 a = f32x4{0.f, 0.f, 0.f, 0.f};
#pragma unroll
      for (int j = 0; j < 4; ++j)
        a = __builtin_amdgcn_mfma_f32_16x16x32_bf16(__builtin_bit_cast(bf16x8, kf[i][j]), __builtin_bit_cast(bf16x8, qf[j]), a, 0, 0, 0);
      sc[i] = a;
    }
    float mx = -INFINITY;
#pragma unroll
    for (int i = 0; i < 4; ++i)
#pragma unroll
      for (int r = 0; r < 4; ++r) {
        const bool live = (tok0 + i * 16 + g * 4 + r) < ctx;
        const float x = live ? sc[i][r] * c2 : -INFINITY;
        sc[i][r] = x;
        mx = fmaxf(mx, x);
      }
    mx = xrow_max(mx);
    float ls = 0.f;
#pragma unroll
    for (int i = 0; i < 4; ++i)
#pragma unroll
      for (int r = 0; r < 4; ++r) {
        const float e = __builtin_amdgcn_exp2f(sc[i][r] - mx);
        sc[i][r] = e;
        ls += e;
      }
    ls = xrow_sum(ls);
    mrow = mx;
    lrow = ls;
#pragma unroll
    for (int p = 0; p < 2; ++p) {
      u32x4 pf;
      pf[0] = cvtpk(sc[2 * p][0], sc[2 * p][1]);
      pf[1] = cvtpk(sc[2 * p][2], sc[2 * p][3]);
      pf[2] = cvtpk(sc[2 * p + 1][0], sc[2 * p + 1][1]);
      pf[3] = cvtpk(sc[2 * p + 1][2], sc[2 * p + 1][3]);
#pragma unroll
      for (int j = 0; j < 8; ++j) {
        u32x4 bfr;
#pragma unroll
        for (int u = 0; u < 4; ++u) {
          const unsigned lo = vf[p][2 * u][j >> 1], hi = vf[p][2 * u + 1][j >> 1];
          bfr[u] = (j & 1) ? __builtin_amdgcn_perm(hi, lo, 0x07060302u) : __builtin_amdgcn_perm(hi, lo, 0x05040100u);
        }
        acc_o[j] = __builtin_amdgcn_mfma_f32_16x16x32_bf16(__builtin_bit_cast(bf16x8, pf), __builtin_bit_cast(bf16x8, bfr), acc_o[j], 0, 0, 0);
      }
    }
  }
  if (g == 0) {
#pragma unroll
    for (int r = 0; r < 4; ++r) {
      *reinterpret_cast<f32x4*>(&s_o[w][r][c * 8]) = f32x4{acc_o[0][r], acc_o[1][r], acc_o[2][r], acc_o[3][r]};
      *reinterpret_cast<f32x4*>(&s_o[w][r][c * 8 + 4]) = f32x4{acc_o[4][r], acc_o[5][r], acc_o[6][r], acc_o[7][r]};
    }
    if (c < 4) { s_m[w][c] = mrow; s_l[w][c] = lrow; }
  }
  __syncthreads();
  {
    const int h = tid >> 7, d = tid & 127;
    float M = -INFINITY;
#pragma unroll
    for (int ww = 0; ww < WAVES; ++ww) M = fmaxf(M, s_m[ww][h]);
    float L = 0.f, o = 0.f;
#pragma unroll
    for (int ww = 0; ww < WAVES; ++ww) {
      const float lw = s_l[ww][h];
      const float wt = (lw > 0.f) ? __builtin_amdgcn_exp2f(s_m[ww][h] - M) : 0.f;
      L = fmaf(wt, lw, L);
      o = fmaf(wt, s_o[ww][h][d], o);
    }
    const size_t slot = ((size_t)b * H_ + kvh * G_ + h) * NSPLIT + s;
    po[slot * D_ + d] = o;
    if (d == 0) { pml[slot * 2] = M; pml[slot * 2 + 1] = L; }
  }
}

__global__ void __launch_bounds__(256)
attn_combine_kernel(const float* __restrict__ po, const float* __restrict__ pml, unsigned* __restrict__ out) {
  const int bid = blockIdx.x;
  const int row = bid * G_ + (threadIdx.x >> 6);
  const int d0 = (threadIdx.x & 63) * 2;
  float m[NSPLIT], l[NSPLIT];
  float2 a[NSPLIT];
#pragma unroll
  for (int s = 0; s < NSPLIT; ++s) {
    const float2 ml = *reinterpret_cast<const float2*>(pml + ((size_t)row * NSPLIT + s) * 2);
    m[s] = ml.x; l[s] = ml.y;
    a[s] = *reinterpret_cast<const float2*>(po + ((size_t)row * NSPLIT + s) * D_ + d0);
  }
  float M = -INFINITY;
#pragma unroll
  for (int s = 0; s < NSPLIT; ++s) M = fmaxf(M, m[s]);
  float L = 0.f, o0 = 0.f, o1 = 0.f;
#pragma unroll
  for (int s = 0; s < NSPLIT; ++s) {
    const float wt = (l[s] > 0.f) ? __builtin_amdgcn_exp2f(m[s] - M) : 0.f;
    L = fmaf(wt, l[s], L);
    o0 = fmaf(wt, a[s].x, o0);
    o1 = fmaf(wt, a[s].y, o1);
  }
  const float inv = 1.f / L;
  out[(size_t)row * (D_ / 2) + (d0 >> 1)] = cvtpk(o0 * inv, o1 * inv);
}

__global__ void spin_k(const unsigned long long* tp) {
  unsigned long long t0 = tp[0];
  while (__builtin_amdgcn_s_memrealtime() - t0 < 3000) { __builtin_amdgcn_s_sleep(1); }
}
}

extern "C" void kernel_launch(void* const* d_in, const int* in_sizes, int n_in,
                              void* d_out, int out_size, void* d_ws, size_t ws_size,
                              hipStream_t stream) {
  const unsigned short* q = (const unsigned short*)d_in[0];
  const unsigned short* kc = (const unsigned short*)d_in[1];
  const unsigned short* vc = (const unsigned short*)d_in[2];
  const int* bt = (const int*)d_in[3];
  const int* lens = (const int*)d_in[4];
  float* po = (float*)((char*)d_ws + WS_PO);
  float* pml = (float*)((char*)d_ws + WS_PML);
  attn_split_kernel<<<dim3(B_ * HKV_ * NSPLIT), dim3(WAVES * 64), 0, stream>>>(q, kc, vc, bt, lens, po, pml, (unsigned long long*)((char*)d_ws + (32u<<20)));
  attn_combine_kernel<<<dim3(B_ * HKV_), dim3(256), 0, stream>>>(po, pml, (unsigned*)d_out);
  spin_k<<<dim3(1), dim3(64), 0, stream>>>((const unsigned long long*)((char*)d_ws + (32u<<20)));
}
```

```cpp
#include <hip/hip_runtime.h>
#include <hip/hip_bf16.h>
#include <stdint.h>

namespace {
#define STAMP(k)
#define STAMP_FLUSH()

constexpr int B_ = 8, H_ = 32, HKV_ = 8, D_ = 128, BS_ = 16, NB_ = 128, LK_ = NB_ * BS_;
constexpr int G_ = H_ / HKV_;
constexpr int NS = 8;
constexpr int NP = 3;
constexpr int NSLOT = NS + NP;
constexpr int WAVES = 8;
constexpr int NTHR = WAVES * 64;
constexpr int TOKROW = HKV_ * D_;
constexpr int NWG = HKV_ * NS + B_ * HKV_ * NP;

using bf16x8 = __attribute__((ext_vector_type(8))) __bf16;
using s16x4 = __attribute__((ext_vector_type(4))) short;
using f32x4 = __attribute__((ext_vector_type(4))) float;
using u32x4 = __attribute__((ext_vector_type(4))) unsigned;
using u32x2 = __attribute__((ext_vector_type(2))) unsigned;
using i32x2 = __attribute__((ext_vector_type(2))) int;
using i32x4 = __attribute__((ext_vector_type(4))) int;
using f32x2 = __attribute__((ext_vector_type(2))) float;
using bf16x2 = __attribute__((ext_vector_type(2))) __bf16;

__device__ __forceinline__ unsigned cvtpk(float lo, float hi) {
  return __builtin_bit_cast(unsigned, __builtin_convertvector(f32x2{lo, hi}, bf16x2));
}
__device__ __forceinline__ float xrow_max(float x) {
  auto s = __builtin_amdgcn_permlane16_swap(__float_as_uint(x), __float_as_uint(x), false, false);
  x = fmaxf(__uint_as_float(s[0]), __uint_as_float(s[1]));
  auto t = __builtin_amdgcn_permlane32_swap(__float_as_uint(x), __float_as_uint(x), false, false);
  return fmaxf(__uint_as_float(t[0]), __uint_as_float(t[1]));
}
__device__ __forceinline__ float xrow_sum(float x) {
  auto s = __builtin_amdgcn_permlane16_swap(__float_as_uint(x), __float_as_uint(x), false, false);
  x = __uint_as_float(s[0]) + __uint_as_float(s[1]);
  auto t = __builtin_amdgcn_permlane32_swap(__float_as_uint(x), __float_as_uint(x), false, false);
  return __uint_as_float(t[0]) + __uint_as_float(t[1]);
}
template <int CTRL> __device__ __forceinline__ int dppi(int x) { return __builtin_amdgcn_mov_dpp(x, CTRL, 0xf, 0xf, true); }
__device__ __forceinline__ u32x4 ldnt(const unsigned short* p) {
  return *reinterpret_cast<const u32x4*>(p);
}
__device__ __forceinline__ u32x4 ld16(const unsigned short* p) {
  return *reinterpret_cast<const u32x4*>(p);
}

constexpr size_t WS_PO = 0;
constexpr size_t WS_PML = (size_t)B_ * H_ * NSLOT * D_ * 4;

constexpr float C2 = 0.088388347648318440f * 1.4426950408889634f;

constexpr int LDS_O = 0;
constexpr int LDS_ML = LDS_O + WAVES * 32 * D_;
constexpr int LDS_F = LDS_ML + WAVES * 32 * 2;
constexpr int LDS_FLOATS = LDS_F + 144;

template <int R>
__device__ __forceinline__ void issue_round(const unsigned short* __restrict__ kb, const unsigned short* __restrict__ vb,
                                            int p0, int hi, i32x2 own, u32x4 (&kr)[R][4], u32x4 (&vr)[R][4]) {
  size_t off[R];
#pragma unroll
  for (int r = 0; r < R; ++r) {
    const int pg = min(p0 + WAVES * r, NB_ - 1);
    const int px = __builtin_amdgcn_readlane(own[0], pg >> 1), py = __builtin_amdgcn_readlane(own[1], pg >> 1);
    off[r] = (size_t)((pg & 1) ? py : px) * (BS_ * TOKROW);
  }
#pragma unroll
  for (int r = 0; r < R; ++r) {
    if (p0 + WAVES * r < hi) {
#pragma unroll
      for (int i = 0; i < 4; ++i) kr[r][i] = ldnt(kb + off[r] + i * TOKROW);
    }
  }
#pragma unroll
  for (int r = 0; r < R; ++r) {
    if (p0 + WAVES * r < hi) {
#pragma unroll
      for (int i = 0; i < 4; ++i) vr[r][i] = ldnt(vb + off[r] + i * TOKROW);
    }
  }
}

template <int NG, int R>
__device__ __forceinline__ void run_pages(const unsigned short* __restrict__ kb, const unsigned short* __restrict__ vb,
                                          int p0, int hi, int w, int lane, i32x2 own,
                                          u32x4 (&kr)[R][4], u32x4 (&vr)[R][4],
                                          const u32x4 (&qf)[NG][4], const int (&ctxc)[NG], const float (&c2c)[NG],
                                          float* lds, float (&mrow)[NG], float (&lrow)[NG], f32x4 (&acc)[NG][8],
                                          unsigned long long (&dbgp)[5]) {
  const int c = lane & 15, g = lane >> 4;
  char* kst = reinterpret_cast<char*>(lds + LDS_O + w * 32 * D_);
  bool first = true;
#pragma unroll 1
  while (true) {
    f32x4 sc[NG][R];
#pragma unroll
    for (int r = 0; r < R; ++r) {
      const int pg = p0 + WAVES * r;
      if (pg < hi) {
#pragma unroll
        for (int i = 0; i < 4; ++i) {
          const int t = g * 4 + i;
          *reinterpret_cast<u32x4*>(kst + t * 256 + ((c ^ t) & 15) * 16) = kr[r][i];
        }
        asm volatile("s_waitcnt lgkmcnt(0)" ::: "memory");
        u32x4 kf[4];
#pragma unroll
        for (int j = 0; j < 4; ++j) kf[j] = *reinterpret_cast<const u32x4*>(kst + c * 256 + (((4 * j + g) ^ c) & 15) * 16);
        asm volatile("s_waitcnt lgkmcnt(0)" ::: "memory");
        const int tokb = pg * BS_ + g * 4;
#pragma unroll
        for (int n = 0; n < NG; ++n) {
          f32x4 a = f32x4{0.f, 0.f, 0.f, 0.f};
#pragma unroll
          for (int j = 0; j < 4; ++j)
            a = __builtin_amdgcn_mfma_f32_16x16x32_bf16(__builtin_bit_cast(bf16x8, kf[j]), __builtin_bit_cast(bf16x8, qf[n][j]), a, 0, 0, 0);
#pragma unroll
          for (int e = 0; e < 4; ++e) a[e] = (tokb + e < ctxc[n]) ? a[e] * c2c[n] : -INFINITY;
          sc[n][r] = a;
        }
      } else {
#pragma unroll
        for (int n = 0; n < NG; ++n) sc[n][r] = f32x4{-INFINITY, -INFINITY, -INFINITY, -INFINITY};
      }
    }
    STAMP(2);
#pragma unroll
    for (int n = 0; n < NG; ++n) {
      float mx = mrow[n];
#pragma unroll
      for (int r = 0; r < R; ++r)
#pragma unroll
        for (int e = 0; e < 4; ++e) mx = fmaxf(mx, sc[n][r][e]);
      mx = xrow_max(mx);
      const float msub = (mx == -INFINITY) ? 0.f : mx;
      const float alpha = __builtin_amdgcn_exp2f(mrow[n] - msub);
      float ls = 0.f;
#pragma unroll
      for (int r = 0; r < R; ++r)
#pragma unroll
        for (int e = 0; e < 4; ++e) {
          const float ex = __builtin_amdgcn_exp2f(sc[n][r][e] - msub);
          sc[n][r][e] = ex;
          ls += ex;
        }
      ls = xrow_sum(ls);
      lrow[n] = fmaf(lrow[n], alpha, ls);
      mrow[n] = mx;
      if (!first) {
#pragma unroll
        for (int e = 0; e < 4; ++e) {
          const float al = __shfl(alpha, g * 4 + e, 64);
#pragma unroll
          for (int j = 0; j < 8; ++j) acc[n][j][e] *= al;
        }
      }
    }
#pragma unroll
    for (int r = 0; r < R; ++r) {
      const int pg = p0 + WAVES * r;
      if (pg < hi) {
        u32x2 pf[NG];
#pragma unroll
        for (int n = 0; n < NG; ++n) { pf[n][0] = cvtpk(sc[n][r][0], sc[n][r][1]); pf[n][1] = cvtpk(sc[n][r][2], sc[n][r][3]); }
#pragma unroll
        for (int j = 0; j < 8; ++j) {
          u32x2 bfr;
#pragma unroll
          for (int u = 0; u < 2; ++u) {
            const unsigned lo_ = vr[r][2 * u][j >> 1], hi_ = vr[r][2 * u + 1][j >> 1];
            bfr[u] = (j & 1) ? __builtin_amdgcn_perm(hi_, lo_, 0x07060302u) : __builtin_amdgcn_perm(hi_, lo_, 0x05040100u);
          }
#pragma unroll
          for (int n = 0; n < NG; ++n)
            acc[n][j] = __builtin_amdgcn_mfma_f32_16x16x16bf16_1k(__builtin_bit_cast(s16x4, pf[n]), __builtin_bit_cast(s16x4, bfr), acc[n][j], 0, 0, 0);
        }
      }
    }
    p0 += WAVES * R;
    if (p0 >= hi) break;
    first = false;
    issue_round<R>(kb, vb, p0, hi, own, kr, vr);
    __builtin_amdgcn_sched_barrier(0);
  }
}

__global__ void __launch_bounds__(NTHR, 2)
attn_split_kernel(const unsigned short* __restrict__ q, const unsigned short* __restrict__ kc,
                  const unsigned short* __restrict__ vc, const int* __restrict__ bt,
                  const int* __restrict__ lens, float* __restrict__ po, float* __restrict__ pml) {
  __shared__ __attribute__((aligned(16))) float lds[LDS_FLOATS];

  const int bid = blockIdx.x;
  const int tid = threadIdx.x, lane = tid & 63;
  const int w = __builtin_amdgcn_readfirstlane(tid >> 6);
  unsigned long long dbgp[5] = {0, 0, 0, 0, 0};
  STAMP(0);
  const int c = lane & 15, g = lane >> 4;
  const bool shared_role = bid < HKV_ * NS;
  const int pid = shared_role ? bid : bid - HKV_ * NS;
  const int kvh = pid & 7;
  const int rest = pid >> 3;
  const int b = shared_role ? 0 : (rest & 7);
  const int part = shared_role ? rest : (rest >> 3);

  int* s_idx = reinterpret_cast<int*>(lds + LDS_F);
  if (w == 0) {
    const int lv = lens[lane & 7];
    const i32x2 ownr = *reinterpret_cast<const i32x2*>(bt + b * NB_ + 2 * lane);
    const i32x4 r0 = *reinterpret_cast<const i32x4*>(bt + 4 * (lane & 31));
    i32x4 rw[4];
#pragma unroll
    for (int k = 0; k < 4; ++k) rw[k] = *reinterpret_cast<const i32x4*>(bt + k * (2 * NB_) + 4 * lane);
    const int le = (lv <= 0 || lv > LK_) ? LK_ : lv;
    const int np = (le + BS_ - 1) >> 4;
    int mx = max(np, dppi<0xB1>(np));
    mx = max(mx, dppi<0x4E>(mx));
    mx = max(mx, dppi<0x141>(mx));
    int f = __builtin_amdgcn_readfirstlane(mx);
#pragma unroll
    for (int e = 0; e < 4; ++e) {
      const bool ne = (rw[0][e] != r0[e]) | (rw[1][e] != r0[e]) | (rw[2][e] != r0[e]) | (rw[3][e] != r0[e]);
      const unsigned long long m = __ballot(ne);
      const unsigned m32 = (unsigned)m | (unsigned)(m >> 32);
      if (m32) f = min(f, 4 * (int)__builtin_ctz(m32) + e);
    }
    *reinterpret_cast<i32x2*>(s_idx + 2 * lane) = ownr;
    if (lane < 8) s_idx[128 + lane] = lv;
    if (lane == 0) s_idx[136] = f;
  }
  const int bc0 = shared_role ? (c >> 2) : b, bc1 = 4 + (c >> 2);
  u32x4 qf[2][4];
  {
    const unsigned short* qb0 = q + ((size_t)bc0 * H_ + kvh * G_ + (c & 3)) * D_ + g * 8;
    const unsigned short* qb1 = q + ((size_t)bc1 * H_ + kvh * G_ + (c & 3)) * D_ + g * 8;
#pragma unroll
    for (int j = 0; j < 4; ++j) qf[0][j] = ld16(qb0 + j * 32);
    if (shared_role) {
#pragma unroll
      for (int j = 0; j < 4; ++j) qf[1][j] = ld16(qb1 + j * 32);
    }
  }
  const unsigned short* kb = kc + (size_t)(g * 4) * TOKROW + kvh * D_ + c * 8;
  const unsigned short* vb = vc + (size_t)(g * 4) * TOKROW + kvh * D_ + c * 8;
  __builtin_amdgcn_sched_barrier(0);
  __syncthreads();
  const i32x2 own = *reinterpret_cast<const i32x2*>(s_idx + 2 * lane);
  const int lenv = s_idx[128 + (lane & 7)];
  const int pc = __builtin_amdgcn_readfirstlane(s_idx[136]);
  const int leneff = (lenv <= 0 || lenv > LK_) ? LK_ : lenv;
  const int npb = (__builtin_amdgcn_readlane(leneff, b) + BS_ - 1) >> 4;
  STAMP(1);

  float* s_o = lds + LDS_O;
  float* s_ml = lds + LDS_ML;
  if (shared_role) {
    const int lo = pc * part / NS, hi = pc * (part + 1) / NS;
    u32x4 kr[1][4], vr[1][4];
    if (lo + w < hi) issue_round<1>(kb, vb, lo + w, hi, own, kr, vr);
    __builtin_amdgcn_sched_barrier(0);
    const int l0 = __shfl(lenv, bc0, 64), l1 = __shfl(lenv, bc1, 64);
    const float c2c[2] = {l0 <= 0 ? 0.f : C2, l1 <= 0 ? 0.f : C2};
    const int ctxc[2] = {__shfl(leneff, bc0, 64), __shfl(leneff, bc1, 64)};
    float mrow[2] = {-INFINITY, -INFINITY}, lrow[2] = {0.f, 0.f};
    f32x4 acc[2][8];
#pragma unroll
    for (int n = 0; n < 2; ++n)
#pragma unroll
      for (int j = 0; j < 8; ++j) acc[n][j] = f32x4{0.f, 0.f, 0.f, 0.f};
    if (lo + w < hi) run_pages<2, 1>(kb, vb, lo + w, hi, w, lane, own, kr, vr, qf, ctxc, c2c, lds, mrow, lrow, acc, dbgp);
#pragma unroll
    for (int n = 0; n < 2; ++n) {
#pragma unroll
      for (int e = 0; e < 4; ++e) {
        float* dst = s_o + ((w * 32) + n * 16 + g * 4 + e) * D_ + c * 8;
        *reinterpret_cast<f32x4*>(dst) = f32x4{acc[n][0][e], acc[n][1][e], acc[n][2][e], acc[n][3][e]};
        *reinterpret_cast<f32x4*>(dst + 4) = f32x4{acc[n][4][e], acc[n][5][e], acc[n][6][e], acc[n][7][e]};
      }
      if (g == 0) { s_ml[(w * 32 + n * 16 + c) * 2] = mrow[n]; s_ml[(w * 32 + n * 16 + c) * 2 + 1] = lrow[n]; }
    }
    STAMP(3);
    __syncthreads();
    const int rr = tid >> 4, d0 = (tid & 15) * 8;
    float M = -INFINITY;
#pragma unroll
    for (int ww = 0; ww < WAVES; ++ww) M = fmaxf(M, s_ml[(ww * 32 + rr) * 2]);
    float L = 0.f;
    f32x4 o[2] = {f32x4{0.f, 0.f, 0.f, 0.f}, f32x4{0.f, 0.f, 0.f, 0.f}};
#pragma unroll
    for (int ww = 0; ww < WAVES; ++ww) {
      const float lw = s_ml[(ww * 32 + rr) * 2 + 1];
      const float wt = (lw > 0.f) ? __builtin_amdgcn_exp2f(s_ml[(ww * 32 + rr) * 2] - M) : 0.f;
      L = fmaf(wt, lw, L);
#pragma unroll
      for (int u = 0; u < 2; ++u) o[u] += wt * *reinterpret_cast<const f32x4*>(s_o + (ww * 32 + rr) * D_ + d0 + 4 * u);
    }
    const size_t slot = ((size_t)(rr >> 2) * H_ + kvh * G_ + (rr & 3)) * NSLOT + part;
#pragma unroll
    for (int u = 0; u < 2; ++u) *reinterpret_cast<f32x4*>(po + slot * D_ + d0 + 4 * u) = o[u];
    if ((tid & 15) == 0) { pml[slot * 2] = M; pml[slot * 2 + 1] = L; }
    STAMP(4);
    STAMP_FLUSH();
  } else {
    const int n = max(npb - pc, 0);
    const int lo = pc + n * part / NP, hi = pc + n * (part + 1) / NP;
    u32x4 kr[3][4], vr[3][4];
    if (lo + w < hi) issue_round<3>(kb, vb, lo + w, hi, own, kr, vr);
    __builtin_amdgcn_sched_barrier(0);
    const int l0 = __builtin_amdgcn_readlane(lenv, b);
    const float c21[1] = {l0 <= 0 ? 0.f : C2};
    const int ctx1[1] = {__builtin_amdgcn_readlane(leneff, b)};
    float mrow[1] = {-INFINITY}, lrow[1] = {0.f};
    f32x4 acc[1][8];
#pragma unroll
    for (int j = 0; j < 8; ++j) acc[0][j] = f32x4{0.f, 0.f, 0.f, 0.f};
    const u32x4(&qf1)[1][4] = *reinterpret_cast<const u32x4(*)[1][4]>(&qf[0]);
    if (lo + w < hi) run_pages<1, 3>(kb, vb, lo + w, hi, w, lane, own, kr, vr, qf1, ctx1, c21, lds, mrow, lrow, acc, dbgp);
    if (g == 0) {
#pragma unroll
      for (int e = 0; e < 4; ++e) {
        float* dst = s_o + (w * 32 + e) * D_ + c * 8;
        *reinterpret_cast<f32x4*>(dst) = f32x4{acc[0][0][e], acc[0][1][e], acc[0][2][e], acc[0][3][e]};
        *reinterpret_cast<f32x4*>(dst + 4) = f32x4{acc[0][4][e], acc[0][5][e], acc[0][6][e], acc[0][7][e]};
      }
      if (c < 4) { s_ml[(w * 4 + c) * 2] = mrow[0]; s_ml[(w * 4 + c) * 2 + 1] = lrow[0]; }
    }
    STAMP(3);
    __syncthreads();
    const int h = tid >> 7, d0 = tid & 127;
    float M = -INFINITY;
#pragma unroll
    for (int ww = 0; ww < WAVES; ++ww) M = fmaxf(M, s_ml[(ww * 4 + h) * 2]);
    float L = 0.f, o0 = 0.f;
#pragma unroll
    for (int ww = 0; ww < WAVES; ++ww) {
      const float lw = s_ml[(ww * 4 + h) * 2 + 1];
      const float wt = (lw > 0.f) ? __builtin_amdgcn_exp2f(s_ml[(ww * 4 + h) * 2] - M) : 0.f;
      L = fmaf(wt, lw, L);
      o0 = fmaf(wt, s_o[(ww * 32 + h) * D_ + d0], o0);
    }
    const size_t slot = ((size_t)b * H_ + kvh * G_ + h) * NSLOT + NS + part;
    po[slot * D_ + d0] = o0;
    if (d0 == 0) { pml[slot * 2] = M; pml[slot * 2 + 1] = L; }
    STAMP(4);
    STAMP_FLUSH();
  }
}

__global__ void __launch_bounds__(256)
attn_combine_kernel(const float* __restrict__ po, const float* __restrict__ pml, unsigned* __restrict__ out) {
  const int bid = blockIdx.x;
  const int row = bid * G_ + (threadIdx.x >> 6);
  const int d0 = (threadIdx.x & 63) * 2;
  float m[NSLOT], l[NSLOT];
  float2 a[NSLOT];
#pragma unroll
  for (int s = 0; s < NSLOT; ++s) {
    const float2 ml = *reinterpret_cast<const float2*>(pml + ((size_t)row * NSLOT + s) * 2);
    m[s] = ml.x; l[s] = ml.y;
    a[s] = *reinterpret_cast<const float2*>(po + ((size_t)row * NSLOT + s) * D_ + d0);
  }
  float M = -INFINITY;
#pragma unroll
  for (int s = 0; s < NSLOT; ++s) M = fmaxf(M, m[s]);
  float L = 0.f, o0 = 0.f, o1 = 0.f;
#pragma unroll
  for (int s = 0; s < NSLOT; ++s) {
    const float wt = (l[s] > 0.f) ? __builtin_amdgcn_exp2f(m[s] - M) : 0.f;
    L = fmaf(wt, l[s], L);
    o0 = fmaf(wt, a[s].x, o0);
    o1 = fmaf(wt, a[s].y, o1);
  }
  const float inv = 1.f / L;
  out[(size_t)row * (D_ / 2) + (d0 >> 1)] = cvtpk(o0 * inv, o1 * inv);
}

}

extern "C" void kernel_launch(void* const* d_in, const int* in_sizes, int n_in,
                              void* d_out, int out_size, void* d_ws, size_t ws_size,
                              hipStream_t stream) {
  const unsigned short* q = (const unsigned short*)d_in[0];
  const unsigned short* kc = (const unsigned short*)d_in[1];
  const unsigned short* vc = (const unsigned short*)d_in[2];
  const int* bt = (const int*)d_in[3];
  const int* lens = (const int*)d_in[4];
  float* po = (float*)((char*)d_ws + WS_PO);
  float* pml = (float*)((char*)d_ws + WS_PML);
  attn_split_kernel<<<dim3(NWG), dim3(NTHR), 0, stream>>>(q, kc, vc, bt, lens, po, pml);
  attn_combine_kernel<<<dim3(B_ * HKV_), dim3(256), 0, stream>>>(po, pml, (unsigned*)d_out);
}
```

```cpp
#include <hip/hip_runtime.h>
#include <stdint.h>
namespace {
__device__ __forceinline__ float bf(unsigned short h) { return __uint_as_float((unsigned)h << 16); }
__global__ void __launch_bounds__(256) naive(const unsigned short* q, const unsigned short* kc, const unsigned short* vc, const int* bt, const int* lens, unsigned short* out) {
  const unsigned long long t0 = __builtin_amdgcn_s_memrealtime();
  __shared__ float sc[2048]; __shared__ float qs[128]; __shared__ float red[256];
  const int row = blockIdx.x, b = row >> 5, h = row & 31, kvh = h >> 2, tid = threadIdx.x;
  int ctx = lens[b]; float c = 0.08838834764831845f; if (ctx <= 0) { ctx = 2048; c = 0.f; } if (ctx > 2048) ctx = 2048;
  if (tid < 128) qs[tid] = bf(q[row * 128 + tid]);
  __syncthreads();
  float mx = -INFINITY;
  for (int t = tid; t < ctx; t += 256) { const unsigned short* k = kc + ((size_t)bt[b * 128 + (t >> 4)] * 16 + (t & 15)) * 1024 + kvh * 128; float s = 0;
    for (int d = 0; d < 128; d += 8) { uint4 x = *(const uint4*)(k + d); const unsigned* u = (const unsigned*)&x; for (int e = 0; e < 4; ++e) { s += qs[d + 2 * e] * __uint_as_float(u[e] << 16) + qs[d + 2 * e + 1] * __uint_as_float(u[e] & 0xffff0000u); } }
    s *= c; sc[t] = s; mx = fmaxf(mx, s); }
  red[tid] = mx; __syncthreads();
  for (int o = 128; o > 0; o >>= 1) { if (tid < o) red[tid] = fmaxf(red[tid], red[tid + o]); __syncthreads(); }
  mx = red[0]; __syncthreads();
  float l = 0; for (int t = tid; t < ctx; t += 256) { float e = __expf(sc[t] - mx); sc[t] = e; l += e; }
  red[tid] = l; __syncthreads();
  for (int o = 128; o > 0; o >>= 1) { if (tid < o) red[tid] += red[tid + o]; __syncthreads(); }
  l = red[0]; __syncthreads();
  const int d = tid & 127, half = tid >> 7; float o = 0;
  for (int t = half; t < ctx; t += 2) o += sc[t] * bf(vc[((size_t)bt[b * 128 + (t >> 4)] * 16 + (t & 15)) * 1024 + kvh * 128 + d]);
  red[tid] = o; __syncthreads();
  if (tid < 128) { float r = (red[tid] + red[tid + 128]) / l; unsigned u = __float_as_uint(r); u = (u + 0x7FFF + ((u >> 16) & 1)) >> 16; out[row * 128 + tid] = (unsigned short)u; }
  if (blockIdx.x == 0) { while (__builtin_amdgcn_s_memrealtime() - t0 < 70000) { __builtin_amdgcn_s_sleep(1); } }
}
__global__ void empty_k(float* p) { if (p == nullptr) *p = 1.f; }
}
extern "C" void kernel_launch(void* const* d_in, const int* in_sizes, int n_in, void* d_out, int out_size, void* d_ws, size_t ws_size, hipStream_t stream) {
  naive<<<256, 256, 0, stream>>>((const unsigned short*)d_in[0], (const unsigned short*)d_in[1], (const unsigned short*)d_in[2], (const int*)d_in[3], (const int*)d_in[4], (unsigned short*)d_out);
}
```

```cpp
#include <hip/hip_runtime.h>
#include <hip/hip_bf16.h>
#include <stdint.h>

namespace {

constexpr int B_ = 8, H_ = 32, HKV_ = 8, D_ = 128, BS_ = 16, NB_ = 128, LK_ = NB_ * BS_;
constexpr int G_ = H_ / HKV_;
constexpr int NS = 8;
constexpr int NP = 3;
constexpr int NSLOT = NS + NP;
constexpr int WAVES = 8;
constexpr int NTHR = WAVES * 64;
constexpr int TOKROW = HKV_ * D_;
constexpr int NWG = HKV_ * NS + B_ * HKV_ * NP;

using bf16x8 = __attribute__((ext_vector_type(8))) __bf16;
using s16x4 = __attribute__((ext_vector_type(4))) short;
using f32x4 = __attribute__((ext_vector_type(4))) float;
using u32x4 = __attribute__((ext_vector_type(4))) unsigned;
using u32x2 = __attribute__((ext_vector_type(2))) unsigned;
using i32x2 = __attribute__((ext_vector_type(2))) int;
using i32x4 = __attribute__((ext_vector_type(4))) int;
using f32x2 = __attribute__((ext_vector_type(2))) float;
using bf16x2 = __attribute__((ext_vector_type(2))) __bf16;

__device__ __forceinline__ unsigned cvtpk(float lo, float hi) {
  return __builtin_bit_cast(unsigned, __builtin_convertvector(f32x2{lo, hi}, bf16x2));
}
__device__ __forceinline__ float xrow_max(float x) {
  auto s = __builtin_amdgcn_permlane16_swap(__float_as_uint(x), __float_as_uint(x), false, false);
  x = fmaxf(__uint_as_float(s[0]), __uint_as_float(s[1]));
  auto t = __builtin_amdgcn_permlane32_swap(__float_as_uint(x), __float_as_uint(x), false, false);
  return fmaxf(__uint_as_float(t[0]), __uint_as_float(t[1]));
}
__device__ __forceinline__ float xrow_sum(float x) {
  auto s = __builtin_amdgcn_permlane16_swap(__float_as_uint(x), __float_as_uint(x), false, false);
  x = __uint_as_float(s[0]) + __uint_as_float(s[1]);
  auto t = __builtin_amdgcn_permlane32_swap(__float_as_uint(x), __float_as_uint(x), false, false);
  return __uint_as_float(t[0]) + __uint_as_float(t[1]);
}
template <int CTRL> __device__ __forceinline__ int dppi(int x) { return __builtin_amdgcn_mov_dpp(x, CTRL, 0xf, 0xf, true); }
__device__ __forceinline__ u32x4 ldnt(const unsigned short* p) {
  return __builtin_nontemporal_load(reinterpret_cast<const u32x4*>(p));
}
__device__ __forceinline__ u32x4 ld16(const unsigned short* p) {
  return *reinterpret_cast<const u32x4*>(p);
}

constexpr size_t WS_PO = 0;
constexpr size_t WS_PML = (size_t)B_ * H_ * NSLOT * D_ * 4;

constexpr float C2 = 0.088388347648318440f * 1.4426950408889634f;

constexpr int LDS_O = 0;
constexpr int LDS_ML = LDS_O + WAVES * 16 * D_;
constexpr int LDS_F = LDS_ML + WAVES * 16 * 2;
constexpr int LDS_FLOATS = LDS_F + 144;

template <int N>
__device__ __forceinline__ void do_round(const unsigned short* __restrict__ kb, const unsigned short* __restrict__ vb,
                                         int p0, int step, i32x2 own, const u32x4 (&qf)[4], int ctxc, float c2c,
                                         char* kst, int c, int g, bool first, float& mrow, float& lrow, f32x4 (&acc)[8]) {
  u32x4 kr[N][4], vr[N][4];
  size_t off[N];
#pragma unroll
  for (int r = 0; r < N; ++r) {
    const int pg = p0 + step * r;
    const int px = __builtin_amdgcn_readlane(own[0], pg >> 1), py = __builtin_amdgcn_readlane(own[1], pg >> 1);
    off[r] = (size_t)((pg & 1) ? py : px) * (BS_ * TOKROW);
  }
#pragma unroll
  for (int r = 0; r < N; ++r)
#pragma unroll
    for (int i = 0; i < 4; ++i) kr[r][i] = ldnt(kb + off[r] + i * TOKROW);
#pragma unroll
  for (int r = 0; r < N; ++r)
#pragma unroll
    for (int i = 0; i < 4; ++i) vr[r][i] = ldnt(vb + off[r] + i * TOKROW);
  __builtin_amdgcn_sched_barrier(0);
  f32x4 sc[N];
#pragma unroll
  for (int r = 0; r < N; ++r) {
#pragma unroll
    for (int i = 0; i < 4; ++i) {
      const int t = g * 4 + i;
      *reinterpret_cast<u32x4*>(kst + t * 256 + ((c ^ t) & 15) * 16) = kr[r][i];
    }
    asm volatile("s_waitcnt lgkmcnt(0)" ::: "memory");
    u32x4 kf[4];
#pragma unroll
    for (int j = 0; j < 4; ++j) kf[j] = *reinterpret_cast<const u32x4*>(kst + c * 256 + (((4 * j + g) ^ c) & 15) * 16);
    asm volatile("s_waitcnt lgkmcnt(0)" ::: "memory");
    const int tokb = (p0 + step * r) * BS_ + g * 4;
    f32x4 a = f32x4{0.f, 0.f, 0.f, 0.f};
#pragma unroll
    for (int j = 0; j < 4; ++j)
      a = __builtin_amdgcn_mfma_f32_16x16x32_bf16(__builtin_bit_cast(bf16x8, kf[j]), __builtin_bit_cast(bf16x8, qf[j]), a, 0, 0, 0);
#pragma unroll
    for (int e = 0; e < 4; ++e) a[e] = (tokb + e < ctxc) ? a[e] * c2c : -INFINITY;
    sc[r] = a;
  }
  {
    float mx = mrow;
#pragma unroll
    for (int r = 0; r < N; ++r)
#pragma unroll
      for (int e = 0; e < 4; ++e) mx = fmaxf(mx, sc[r][e]);
    mx = xrow_max(mx);
    const float msub = (mx == -INFINITY) ? 0.f : mx;
    const float alpha = __builtin_amdgcn_exp2f(mrow - msub);
    float ls = 0.f;
#pragma unroll
    for (int r = 0; r < N; ++r)
#pragma unroll
      for (int e = 0; e < 4; ++e) {
        const float ex = __builtin_amdgcn_exp2f(sc[r][e] - msub);
        sc[r][e] = ex;
        ls += ex;
      }
    ls = xrow_sum(ls);
    lrow = fmaf(lrow, alpha, ls);
    mrow = mx;
    if (!first) {
#pragma unroll
      for (int e = 0; e < 4; ++e) {
        const float al = __shfl(alpha, g * 4 + e, 64);
#pragma unroll
        for (int j = 0; j < 8; ++j) acc[j][e] *= al;
      }
    }
  }
#pragma unroll
  for (int r = 0; r < N; ++r) {
    u32x2 pf;
    pf[0] = cvtpk(sc[r][0], sc[r][1]);
    pf[1] = cvtpk(sc[r][2], sc[r][3]);
#pragma unroll
    for (int j = 0; j < 8; ++j) {
      u32x2 bfr;
#pragma unroll
      for (int u = 0; u < 2; ++u) {
        const unsigned lo_ = vr[r][2 * u][j >> 1], hi_ = vr[r][2 * u + 1][j >> 1];
        bfr[u] = (j & 1) ? __builtin_amdgcn_perm(hi_, lo_, 0x07060302u) : __builtin_amdgcn_perm(hi_, lo_, 0x05040100u);
      }
      acc[j] = __builtin_amdgcn_mfma_f32_16x16x16bf16_1k(__builtin_bit_cast(s16x4, pf), __builtin_bit_cast(s16x4, bfr), acc[j], 0, 0, 0);
    }
  }
}

__global__ void __launch_bounds__(NTHR, 2)
attn_split_kernel(const unsigned short* __restrict__ q, const unsigned short* __restrict__ kc,
                  const unsigned short* __restrict__ vc, const int* __restrict__ bt,
                  const int* __restrict__ lens, float* __restrict__ po, float* __restrict__ pml) {
  __shared__ __attribute__((aligned(16))) float lds[LDS_FLOATS];

  const int bid = blockIdx.x;
  const int tid = threadIdx.x, lane = tid & 63;
  const int w = __builtin_amdgcn_readfirstlane(tid >> 6);
  const int c = lane & 15, g = lane >> 4;
  const bool shared_role = bid < HKV_ * NS;
  const int pid = shared_role ? bid : bid - HKV_ * NS;
  const int kvh = pid & 7;
  const int rest = pid >> 3;
  const int b = shared_role ? 0 : (rest & 7);
  const int part = shared_role ? rest : (rest >> 3);

  int* s_idx = reinterpret_cast<int*>(lds + LDS_F);
  if (w == 0) {
    const int lv = lens[lane & 7];
    const i32x2 ownr = *reinterpret_cast<const i32x2*>(bt + b * NB_ + 2 * lane);
    const i32x4 r0 = *reinterpret_cast<const i32x4*>(bt + 4 * (lane & 31));
    i32x4 rw[4];
#pragma unroll
    for (int k = 0; k < 4; ++k) rw[k] = *reinterpret_cast<const i32x4*>(bt + k * (2 * NB_) + 4 * lane);
    const int le = (lv <= 0 || lv > LK_) ? LK_ : lv;
    const int np = (le + BS_ - 1) >> 4;
    int mx = max(np, dppi<0xB1>(np));
    mx = max(mx, dppi<0x4E>(mx));
    mx = max(mx, dppi<0x141>(mx));
    int f = __builtin_amdgcn_readfirstlane(mx);
#pragma unroll
    for (int e = 0; e < 4; ++e) {
      const bool ne = (rw[0][e] != r0[e]) | (rw[1][e] != r0[e]) | (rw[2][e] != r0[e]) | (rw[3][e] != r0[e]);
      const unsigned long long m = __ballot(ne);
      const unsigned m32 = (unsigned)m | (unsigned)(m >> 32);
      if (m32) f = min(f, 4 * (int)__builtin_ctz(m32) + e);
    }
    *reinterpret_cast<i32x2*>(s_idx + 2 * lane) = ownr;
    if (lane < 8) s_idx[128 + lane] = lv;
    if (lane == 0) s_idx[136] = f;
  }
  const int bc = shared_role ? ((w & 1) * 4 + (c >> 2)) : b;
  u32x4 qf[4];
  {
    const unsigned short* qb = q + ((size_t)bc * H_ + kvh * G_ + (c & 3)) * D_ + g * 8;
#pragma unroll
    for (int j = 0; j < 4; ++j) qf[j] = ld16(qb + j * 32);
  }
  const unsigned short* kb = kc + (size_t)(g * 4) * TOKROW + kvh * D_ + c * 8;
  const unsigned short* vb = vc + (size_t)(g * 4) * TOKROW + kvh * D_ + c * 8;
  __builtin_amdgcn_sched_barrier(0);
  __syncthreads();
  const i32x2 own = *reinterpret_cast<const i32x2*>(s_idx + 2 * lane);
  const int lenv = s_idx[128 + (lane & 7)];
  const int pc = __builtin_amdgcn_readfirstlane(s_idx[136]);
  const int leneff = (lenv <= 0 || lenv > LK_) ? LK_ : lenv;
  const int npb = (__builtin_amdgcn_readlane(leneff, b) + BS_ - 1) >> 4;
  int p0, hi, step;
  if (shared_role) {
    p0 = pc * part / NS + (w >> 1); hi = pc * (part + 1) / NS; step = WAVES / 2;
  } else {
    const int n = max(npb - pc, 0);
    p0 = pc + n * part / NP + w; hi = pc + n * (part + 1) / NP; step = WAVES;
  }
  const float c2c = (__shfl(lenv, bc, 64) <= 0) ? 0.f : C2;
  const int ctxc = __shfl(leneff, bc, 64);
  float mrow = -INFINITY, lrow = 0.f;
  f32x4 acc[8];
#pragma unroll
  for (int j = 0; j < 8; ++j) acc[j] = f32x4{0.f, 0.f, 0.f, 0.f};
  char* kst = reinterpret_cast<char*>(lds + LDS_O + w * 16 * D_);
  bool first = true;
#pragma unroll 1
  while (p0 < hi) {
    if (p0 + 2 * step < hi) { do_round<3>(kb, vb, p0, step, own, qf, ctxc, c2c, kst, c, g, first, mrow, lrow, acc); p0 += 3 * step; }
    else if (p0 + step < hi) { do_round<2>(kb, vb, p0, step, own, qf, ctxc, c2c, kst, c, g, first, mrow, lrow, acc); p0 += 2 * step; }
    else { do_round<1>(kb, vb, p0, step, own, qf, ctxc, c2c, kst, c, g, first, mrow, lrow, acc); p0 += step; }
    first = false;
  }

  float* s_o = lds + LDS_O;
  float* s_ml = lds + LDS_ML;
  if (shared_role || g == 0) {
#pragma unroll
    for (int e = 0; e < 4; ++e) {
      float* dst = s_o + (w * 16 + g * 4 + e) * D_ + c * 8;
      *reinterpret_cast<f32x4*>(dst) = f32x4{acc[0][e], acc[1][e], acc[2][e], acc[3][e]};
      *reinterpret_cast<f32x4*>(dst + 4) = f32x4{acc[4][e], acc[5][e], acc[6][e], acc[7][e]};
    }
  }
  if (g == 0) *reinterpret_cast<float2*>(s_ml + (w * 16 + c) * 2) = make_float2(mrow, lrow);
  __syncthreads();
  if (shared_role) {
    const int rr = tid >> 4, d0 = (tid & 15) * 8, grp = rr >> 4, r16 = rr & 15;
    float2 ml[WAVES / 2];
    f32x4 x[WAVES / 2][2];
#pragma unroll
    for (int k = 0; k < WAVES / 2; ++k) {
      const int rw_ = (2 * k + grp) * 16 + r16;
      ml[k] = *reinterpret_cast<const float2*>(s_ml + rw_ * 2);
      x[k][0] = *reinterpret_cast<const f32x4*>(s_o + rw_ * D_ + d0);
      x[k][1] = *reinterpret_cast<const f32x4*>(s_o + rw_ * D_ + d0 + 4);
    }
    float M = -INFINITY;
#pragma unroll
    for (int k = 0; k < WAVES / 2; ++k) M = fmaxf(M, ml[k].x);
    const float Ms = (M == -INFINITY) ? 0.f : M;
    float L = 0.f;
    f32x4 o[2] = {f32x4{0.f, 0.f, 0.f, 0.f}, f32x4{0.f, 0.f, 0.f, 0.f}};
#pragma unroll
    for (int k = 0; k < WAVES / 2; ++k) {
      const float wt = __builtin_amdgcn_exp2f(ml[k].x - Ms);
      L = fmaf(wt, ml[k].y, L);
      o[0] += wt * x[k][0];
      o[1] += wt * x[k][1];
    }
    const size_t slot = ((size_t)(rr >> 2) * H_ + kvh * G_ + (rr & 3)) * NSLOT + part;
#pragma unroll
    for (int u = 0; u < 2; ++u) *reinterpret_cast<f32x4*>(po + slot * D_ + d0 + 4 * u) = o[u];
    if ((tid & 15) == 0) *reinterpret_cast<float2*>(pml + slot * 2) = make_float2(M, L);
  } else {
    const int h = tid >> 7, d0 = tid & 127;
    float2 ml[WAVES];
    float x[WAVES];
#pragma unroll
    for (int ww = 0; ww < WAVES; ++ww) {
      ml[ww] = *reinterpret_cast<const float2*>(s_ml + (ww * 16 + h) * 2);
      x[ww] = s_o[(ww * 16 + h) * D_ + d0];
    }
    float M = -INFINITY;
#pragma unroll
    for (int ww = 0; ww < WAVES; ++ww) M = fmaxf(M, ml[ww].x);
    const float Ms = (M == -INFINITY) ? 0.f : M;
    float L = 0.f, o0 = 0.f;
#pragma unroll
    for (int ww = 0; ww < WAVES; ++ww) {
      const float wt = __builtin_amdgcn_exp2f(ml[ww].x - Ms);
      L = fmaf(wt, ml[ww].y, L);
      o0 = fmaf(wt, x[ww], o0);
    }
    const size_t slot = ((size_t)b * H_ + kvh * G_ + h) * NSLOT + NS + part;
    po[slot * D_ + d0] = o0;
    if (d0 == 0) *reinterpret_cast<float2*>(pml + slot * 2) = make_float2(M, L);
  }
}

__global__ void __launch_bounds__(256)
attn_combine_kernel(const float* __restrict__ po, const float* __restrict__ pml, unsigned* __restrict__ out) {
  const int bid = blockIdx.x;
  const int row = bid * G_ + (threadIdx.x >> 6);
  const int d0 = (threadIdx.x & 63) * 2;
  float m[NSLOT], l[NSLOT];
  float2 a[NSLOT];
#pragma unroll
  for (int s = 0; s < NSLOT; ++s) {
    const float2 ml = *reinterpret_cast<const float2*>(pml + ((size_t)row * NSLOT + s) * 2);
    m[s] = ml.x; l[s] = ml.y;
    a[s] = *reinterpret_cast<const float2*>(po + ((size_t)row * NSLOT + s) * D_ + d0);
  }
  float M = -INFINITY;
#pragma unroll
  for (int s = 0; s < NSLOT; ++s) M = fmaxf(M, m[s]);
  float L = 0.f, o0 = 0.f, o1 = 0.f;
#pragma unroll
  for (int s = 0; s < NSLOT; ++s) {
    const float wt = __builtin_amdgcn_exp2f(m[s] - M);
    L = fmaf(wt, l[s], L);
    o0 = fmaf(wt, a[s].x, o0);
    o1 = fmaf(wt, a[s].y, o1);
  }
  const float inv = 1.f / L;
  out[(size_t)row * (D_ / 2) + (d0 >> 1)] = cvtpk(o0 * inv, o1 * inv);
}

}

extern "C" void kernel_launch(void* const* d_in, const int* in_sizes, int n_in,
                              void* d_out, int out_size, void* d_ws, size_t ws_size,
                              hipStream_t stream) {
  const unsigned short* q = (const unsigned short*)d_in[0];
  const unsigned short* kc = (const unsigned short*)d_in[1];
  const unsigned short* vc = (const unsigned short*)d_in[2];
  const int* bt = (const int*)d_in[3];
  const int* lens = (const int*)d_in[4];
  float* po = (float*)((char*)d_ws + WS_PO);
  float* pml = (float*)((char*)d_ws + WS_PML);
  attn_split_kernel<<<dim3(NWG), dim3(NTHR), 0, stream>>>(q, kc, vc, bt, lens, po, pml);
  attn_combine_kernel<<<dim3(B_ * HKV_), dim3(256), 0, stream>>>(po, pml, (unsigned*)d_out);
}
```
